```python
import torch

def _softmax(x, axis=-1):
    return torch.softmax(x, dim=axis)
def _gelu(x):
    return torch.nn.functional.gelu(x, approximate="tanh")
def _silu(x):
    return torch.nn.functional.silu(x)
def _sigmoid(x):
    return torch.sigmoid(x)
def _layernorm(x, axis=-1, eps=1e-5):
    m = x.mean(dim=axis, keepdim=True)
    v = x.var(dim=axis, keepdim=True, unbiased=False)
    return (x - m) / torch.sqrt(v + eps)
def _scatter_add(tgt, idx, src, axis=0):
    return tgt.clone().index_add_(axis, idx, src)
def _segment_sum(src, seg, n):
    out = torch.zeros((n,) + src.shape[1:], dtype=src.dtype)
    return out.index_add_(0, seg, src)
def _pad(x, pw):
    flat = tuple(v for pair in reversed(pw) for v in pair)
    return torch.nn.functional.pad(x, flat)

kernel_name = 'gated_mix__linear_kernel_hedgehog__mixer_channel_mlp__prompt'

def setup_inputs():

    _w = (lambda a: (lambda t: t.to(torch.bfloat16) if t.is_floating_point() else t)(torch.as_tensor(a)))
    def _A_setup():
        B, S, D, H = 1, 2048, 512, 32
        Dh = D // H
        return {
            "x": _w((lambda *s: torch.randn(*s, dtype=torch.bfloat16) * 0.05)(B, S, D)),
            "wqkv": _w((lambda *s: torch.randn(*s, dtype=torch.bfloat16) * 0.8)(D, 3 * D)),
            "w_phi": _w((lambda *s: torch.randn(*s, dtype=torch.bfloat16) * 0.8)(H, Dh, Dh) * (Dh ** -0.5)),
            "wo": _w((lambda *s: torch.randn(*s, dtype=torch.bfloat16) * 0.1)(D, D)),
        }
    _A_in = _A_setup()

    def _B_setup():
        B, S, D, F = 1, 2048, 512, 1376
        return {
            "x": _w((lambda *s: torch.randn(*s, dtype=torch.bfloat16) * 0.05)(B, S, D)),
            "w1": _w((lambda *s: torch.randn(*s, dtype=torch.bfloat16) * 0.8)(D, F) * (D ** -0.5)),
            "b1": _w((lambda *s: torch.randn(*s, dtype=torch.bfloat16) * 0.4)(F)),
            "w2": _w((lambda *s: torch.randn(*s, dtype=torch.bfloat16) * 0.05)(F, D) * (F ** -0.5)),
            "b2": _w((lambda *s: torch.randn(*s, dtype=torch.bfloat16) * 0.05)(D)),
        }
    _B_in = _B_setup()

    return {
        "A_x": _A_in["x"],
        "A_wqkv": _A_in["wqkv"],
        "A_w_phi": _A_in["w_phi"],
        "A_wo": _A_in["wo"],
        "B_x": _B_in["x"],
        "B_w1": _B_in["w1"],
        "B_b1": _B_in["b1"],
        "B_w2": _B_in["w2"],
        "B_b2": _B_in["b2"],
        "gate_w": _w((lambda *s: torch.randn(*s, dtype=torch.bfloat16) * 0.05)(512) * 16.0),
    }

def reference(A_x, A_wqkv, A_w_phi, A_wo, B_x, B_w1, B_b1, B_w2, B_b2, gate_w):
    def _A_ref(x, wqkv, w_phi, wo):
        B, S, D = x.shape
        H = 32; Dh = D // H
        qkv = (lambda x, *d: x.permute(*d))((x @ wqkv).reshape(B, S, 3, H, Dh), 2, 0, 3, 1, 4)
        q, k, v = qkv[0], qkv[1], qkv[2]
        pq = torch.einsum("bhsd,hdf->bhsf", q, w_phi)
        pk = torch.einsum("bhsd,hdf->bhsf", k, w_phi)
        phi_q = torch.concatenate([_softmax(pq, axis=-1), _softmax(-pq, axis=-1)], axis=-1)
        phi_k = torch.concatenate([_softmax(pk, axis=-1), _softmax(-pk, axis=-1)], axis=-1)
        kv = torch.einsum("bhsf,bhsd->bhfd", phi_k, v)
        z  = torch.einsum("bhsf->bhf", phi_k)
        num = torch.einsum("bhqf,bhfd->bhqd", phi_q, kv)
        den = torch.einsum("bhqf,bhf->bhq", phi_q, z)[..., None] + 1e-6
        o = num / den
        return (lambda x, *d: x.permute(*d))(o, 0, 2, 1, 3).reshape(B, S, D) @ wo

    def _B_ref(x, w1, b1, w2, b2):
        y = _layernorm(x, axis=-1)
        h = _gelu(torch.einsum("bsd,df->bsf", y, w1) + b1)
        return torch.einsum("bsf,fd->bsd", h, w2) + b2

    x = A_x
    g = _sigmoid((x * gate_w).sum(-1, keepdims=True))
    a = _A_ref(x, A_wqkv, A_w_phi, A_wo)
    b = _B_ref(x, B_w1, B_b1, B_w2, B_b2)
    return g * a + (1 - g) * b
```

```cpp
#include <hip/hip_runtime.h>
#include <hip/hip_bf16.h>
typedef unsigned short u16;
__device__ __forceinline__ float b2f(u16 b){ return __uint_as_float(((unsigned)b)<<16); }
__device__ __forceinline__ u16 f2b(float f){ unsigned u=__float_as_uint(f); u+=0x7fffu+((u>>16)&1u); return (u16)(u>>16); }
__device__ __forceinline__ float rb(float f){ return b2f(f2b(f)); }
constexpr int S=2048,D=512,H=32,DH=16,F=1376;

__global__ void k_stats(const u16* x,const u16* gw,float* st){
  int s=blockIdx.x; int l=threadIdx.x;
  float sum=0,dot=0;
  for(int k=l;k<D;k+=64){ float v=b2f(x[s*D+k]); sum+=v; dot+=v*b2f(gw[k]); }
  for(int m=32;m>=1;m>>=1){ sum+=__shfl_xor(sum,m); dot+=__shfl_xor(dot,m);}
  float mean=sum/D; float var=0;
  for(int k=l;k<D;k+=64){ float v=b2f(x[s*D+k])-mean; var+=v*v; }
  for(int m=32;m>=1;m>>=1){ var+=__shfl_xor(var,m);}
  var/=D;
  if(l==0){ st[s*4+0]=mean; st[s*4+1]=rsqrtf(var+1e-5f); st[s*4+2]=1.f/(1.f+__expf(-dot)); }
}
__global__ void k_qkv(const u16* x,const u16* w,float* qkv){
  int n=blockIdx.x*256+threadIdx.x; int s=blockIdx.y;
  float a=0; for(int k=0;k<D;k++) a+=b2f(x[s*D+k])*b2f(w[k*1536+n]);
  qkv[s*1536+n]=rb(a);
}
__global__ void k_h(const u16* x,const u16* w1,const u16* b1,const float* st,float* h){
  int n=blockIdx.x*256+threadIdx.x; int s=blockIdx.y; if(n>=F) return;
  float m=st[s*4],r=st[s*4+1];
  float a=0; for(int k=0;k<D;k++) a+=rb((b2f(x[s*D+k])-m)*r)*b2f(w1[k*F+n]);
  a+=b2f(b1[n]);
  float u=0.7978845608028654f*(a+0.044715f*a*a*a);
  h[s*F+n]=rb(0.5f*a*(1.f+tanhf(u)));
}
__global__ void k_phi(const float* qkv,const u16* wphi,float* phi){
  int idx=blockIdx.x*256+threadIdx.x;
  int h=idx%H; int s=(idx/H)%S; int which=idx/(H*S);
  float p[16];
  for(int f=0;f<16;f++){ float a=0; for(int d=0;d<16;d++) a+=qkv[s*1536+which*512+h*16+d]*b2f(wphi[h*256+d*16+f]); p[f]=a; }
  float mx=p[0],mn=p[0]; for(int f=1;f<16;f++){mx=fmaxf(mx,p[f]); mn=fminf(mn,p[f]);}
  float sp=0,sn=0; for(int f=0;f<16;f++){ sp+=__expf(p[f]-mx); sn+=__expf(mn-p[f]); }
  float* o=phi+(size_t)idx*32;
  for(int f=0;f<16;f++){ o[f]=rb(__expf(p[f]-mx)/sp); o[16+f]=rb(__expf(mn-p[f])/sn); }
}
__global__ void k_kv(const float* qkv,const float* phi,float* kv){
  int h=blockIdx.x; int t=threadIdx.x;
  if(t>=32*17) return; int f=t/17,d=t%17;
  float a=0; for(int s=0;s<S;s++){ float pk=phi[((size_t)(S+s)*H+h)*32+f]; float v= d<16? qkv[s*1536+1024+h*16+d]:1.f; a+=pk*v; }
  kv[(h*32+f)*17+d]=a;
}
__global__ void k_o(const float* phi,const float* kv,float* o){
  int idx=blockIdx.x*256+threadIdx.x;
  int d=idx%16,h=(idx/16)%H,s=idx/512;
  float num=0,den=0; for(int f=0;f<32;f++){ float pq=phi[((size_t)s*H+h)*32+f]; num+=pq*kv[(h*32+f)*17+d]; den+=pq*kv[(h*32+f)*17+16]; }
  o[idx]=rb(num/(den+1e-6f));
}
__global__ void k_out(const float* o,const float* h,const u16* wo,const u16* w2,const u16* b2,const float* st,u16* out){
  int n=blockIdx.x*256+threadIdx.x; int s=blockIdx.y;
  float a=0; for(int k=0;k<D;k++) a+=o[s*D+k]*b2f(wo[k*D+n]);
  float b=0; for(int k=0;k<F;k++) b+=h[s*F+k]*b2f(w2[k*D+n]);
  b+=b2f(b2[n]); float g=st[s*4+2];
  out[s*D+n]=f2b(g*a+(1.f-g)*b);
}
extern "C" void kernel_launch(void* const* d_in, const int* in_sizes, int n_in, void* d_out, int out_size, void* d_ws, size_t ws_size, hipStream_t stream){
  const u16* x=(const u16*)d_in[0]; const u16* wqkv=(const u16*)d_in[1]; const u16* wphi=(const u16*)d_in[2]; const u16* wo=(const u16*)d_in[3];
  const u16* w1=(const u16*)d_in[5]; const u16* b1=(const u16*)d_in[6]; const u16* w2=(const u16*)d_in[7]; const u16* b2=(const u16*)d_in[8]; const u16* gw=(const u16*)d_in[9];
  float* ws=(float*)d_ws;
  float* st=ws; float* qkv=st+S*4; float* h=qkv+S*1536; float* phi=h+S*F; float* kv=phi+(size_t)2*S*H*32; float* o=kv+32*32*17+64;
  k_stats<<<S,64,0,stream>>>(x,gw,st);
  k_qkv<<<dim3(6,S),256,0,stream>>>(x,wqkv,qkv);
  k_h<<<dim3(6,S),256,0,stream>>>(x,w1,b1,st,h);
  k_phi<<<2*S*H/256,256,0,stream>>>(qkv,wphi,phi);
  k_kv<<<H,576,0,stream>>>(qkv,phi,kv);
  k_o<<<S*512/256,256,0,stream>>>(phi,kv,o);
  k_out<<<dim3(2,S),256,0,stream>>>(o,h,wo,w2,b2,st,(u16*)d_out);
}
```

```cpp
#include <hip/hip_runtime.h>

typedef unsigned short u16;
typedef unsigned int u32;
typedef unsigned long long u64;
typedef unsigned char u8;
typedef __bf16 bf2 __attribute__((ext_vector_type(2)));
typedef __bf16 bf8 __attribute__((ext_vector_type(8)));
typedef short s4 __attribute__((ext_vector_type(4)));
typedef float f2 __attribute__((ext_vector_type(2)));
typedef float f4 __attribute__((ext_vector_type(4)));
typedef float f16v __attribute__((ext_vector_type(16)));
typedef u32 v4u __attribute__((ext_vector_type(4)));
typedef u32 v2u __attribute__((ext_vector_type(2)));
#define GAS __attribute__((address_space(1)))
#define LAS __attribute__((address_space(3)))
#define DI __device__ __forceinline__
#define RLX_AGENT __ATOMIC_RELAXED, __HIP_MEMORY_SCOPE_AGENT

namespace {

constexpr int DM = 512;
constexpr int NKS = 118;
constexpr u32 WS_EPOCH = 0;
constexpr u32 WS_FKV = 4096;
constexpr u32 WS_FH1 = WS_FKV + 8192;
constexpr u32 WS_F2 = WS_FH1 + 8192;
constexpr u32 WS_FS = WS_F2 + 16384;
constexpr u32 WS_FC = WS_FS + 16384;
constexpr u32 WS_ST = WS_FS + 32768;
constexpr u32 WS_CS = WS_ST + 32768;
constexpr u32 WS_PART = 262144;
constexpr u32 WS_A = WS_PART + 1024u * 640u;
constexpr u32 WS_END = WS_A + 64u * NKS * 1024u;
constexpr u32 OOB = 0x7FFFFFF0u;
static_assert(WS_CS + 49152 <= WS_PART, "ws map");
constexpr u32 RB = 16384;
constexpr u32 L_EP = 0;
constexpr u32 L_B2 = L_EP + 1024;
constexpr u32 L_T3 = L_B2 + 1024;
constexpr u32 L_CS = L_T3 + 2048;
constexpr u32 L_CB = L_CS + 6144;
constexpr u32 L_KVT = L_CS;
constexpr u32 L_ZT = L_KVT + 4096;
constexpr u32 L_Z0 = L_CB + 3072;
constexpr u32 L_END = L_Z0 + 512;
static_assert(L_END + 512 <= RB && RB + 18 * 8192 <= 163840, "lds map");
constexpr u32 LDS_BYTES = 163840;
constexpr u32 SPIN_CAP = 1u << 16;
constexpr u32 slot(int s) { return RB + (u32)s * 8192u; }
constexpr u32 ring(int u) { return slot(u % 17); }
constexpr u32 slotB(int s) { return s == 0 ? slot(17) : ring(31 + s); }
constexpr u32 slot2(int i) { return i < 10 ? ring(39 + i) : i < 18 ? slotB(i - 10) : slot2(i - 18); }

DI u32 pk2(float a, float b) { f2 v = {a, b}; bf2 r = __builtin_convertvector(v, bf2); return __builtin_bit_cast(u32, r); }
DI float dot2(u32 a, u32 b, float c) { return __builtin_amdgcn_fdot2_f32_bf16(__builtin_bit_cast(bf2, a), __builtin_bit_cast(bf2, b), c, false); }
DI float sum8(v4u a) {
  const u32 a0 = a.x, a1 = a.y, a2 = a.z, a3 = a.w;
  return dot2(a1, 0x3F803F80u, dot2(a0, 0x3F803F80u, 0.f)) + dot2(a3, 0x3F803F80u, dot2(a2, 0x3F803F80u, 0.f));
}
DI f16v mfma32(v4u a, v4u b, f16v c) { return __builtin_amdgcn_mfma_f32_32x32x16_bf16(__builtin_bit_cast(bf8, a), __builtin_bit_cast(bf8, b), c, 0, 0, 0); }
DI f4 mfma16(v4u a, v4u b, f4 c) { return __builtin_amdgcn_mfma_f32_16x16x32_bf16(__builtin_bit_cast(bf8, a), __builtin_bit_cast(bf8, b), c, 0, 0, 0); }
DI float bf2f(u32 b) { return __builtin_bit_cast(float, b << 16); }
DI float bfhi(u32 b) { return __builtin_bit_cast(float, b & 0xFFFF0000u); }
DI v4u lds128(u32 a) { return *(const LAS v4u*)(size_t)a; }
DI f4 ldsf4(u32 a) { return *(const LAS f4*)(size_t)a; }
DI void sts128(u32 a, v4u v) { *(LAS v4u*)(size_t)a = v; }
DI void stsf4(u32 a, f4 v) { *(LAS f4*)(size_t)a = v; }
DI void sts64(u32 a, v2u v) { *(LAS v2u*)(size_t)a = v; }
DI void sts32(u32 a, float v) { *(LAS float*)(size_t)a = v; }
DI v2u tr64(u32 a) { return __builtin_bit_cast(v2u, __builtin_amdgcn_ds_read_tr16_b64_v4i16((LAS s4*)(size_t)a)); }
template <int ROWB> DI v4u tr_frag(u32 a) { const v2u u0 = tr64(a), u1 = tr64(a + 4 * ROWB); return v4u{u0.x, u0.y, u1.x, u1.y}; }
template <u32 DST, int IMM, int NT = 0> DI void glds(const void* sbase, u32 voff, u32 wb) {
  if constexpr (NT == 1)
    asm volatile("s_add_u32 m0, %2, %3\n\ts_nop 0\n\tglobal_load_lds_dwordx4 %0, %1 offset:%4 nt"
                 :: "v"(voff), "s"(sbase), "s"(wb), "i"(DST - (u32)IMM), "i"(IMM) : "memory", "scc");
  else if constexpr (NT == 2)
    asm volatile("s_add_u32 m0, %2, %3\n\ts_nop 0\n\tglobal_load_lds_dwordx4 %0, %1 offset:%4 sc1"
                 :: "v"(voff), "s"(sbase), "s"(wb), "i"(DST - (u32)IMM), "i"(IMM) : "memory", "scc");
  else
    asm volatile("s_add_u32 m0, %2, %3\n\ts_nop 0\n\tglobal_load_lds_dwordx4 %0, %1 offset:%4"
                 :: "v"(voff), "s"(sbase), "s"(wb), "i"(DST - (u32)IMM), "i"(IMM) : "memory", "scc");
}
#define NTX 0
#define NTW 0
#define NTU 32
DI void glds_sc1(const void* sbase, u32 voff, u32 dst) {
  asm volatile("s_mov_b32 m0, %2\n\ts_nop 0\n\tglobal_load_lds_dwordx4 %0, %1 sc1" :: "v"(voff), "s"(sbase), "s"(dst) : "memory");
}
DI v4u gload_sc1(const void* sbase, u32 voff) {
  v4u r; asm volatile("global_load_dwordx4 %0, %1, %2 sc1" : "=v"(r) : "v"(voff), "s"(sbase) : "memory"); return r;
}
template <int N> DI void wait_bar() { asm volatile("s_waitcnt vmcnt(%0) lgkmcnt(0)\n\ts_barrier" :: "i"(N) : "memory"); }
template <int N> DI void wait_for(v4u& a) { asm volatile("s_waitcnt vmcnt(%1)" : "+v"(a) : "i"(N) : "memory"); }
#define BAR_L() asm volatile("s_waitcnt lgkmcnt(0)\n\ts_barrier" ::: "memory")
#define SCHED() __builtin_amdgcn_sched_barrier(0)
#define OPAQUE(v) asm volatile("" : "+v"(v))
constexpr int AUX_SC1 = 16;
DI u32 umin(u32 a, u32 b) { return a < b ? a : b; }

struct Ctx {
  const u8 *xb0, *xb1, *wb, *w2, *wo;
  u8* ws;
  __amdgpu_buffer_rsrc_t rs;
  int lane, w, t, pj, wm, wn;
  u32 wb1k;
  u32 rot1, xoff, woff, woffB, vx, vw;
  u32 w2off, whi, rot2;
};

DI void poll_at(const Ctx& c, u32 off, u64 tag) {
  const GAS u64* f = (const GAS u64*)(c.ws + off);
  for (u32 spins = 0; spins < SPIN_CAP; ++spins) {
    const u64 v = __hip_atomic_load(f, RLX_AGENT);
    if (__all(v == tag)) break;
    __builtin_amdgcn_s_sleep(1);
  }
  __builtin_amdgcn_fence(__ATOMIC_ACQUIRE, "wavefront");
}
DI u64 read_tag() { const v2u e = *(const LAS v2u*)(size_t)L_EP; return (((u64)e.y << 32) | e.x) + 1ull; }
DI void raise_wave_flag(const Ctx& c, u32 off, u64 tag) {
  __builtin_amdgcn_raw_buffer_store_b64(v2u{(u32)tag, (u32)(tag >> 32)}, c.rs, (int)(c.lane == 0 ? off : OOB), 0, AUX_SC1);
}
DI v2u fetch_flag(const Ctx& c, u32 off) {
  v2u r; asm volatile("global_load_dwordx2 %0, %1, %2 sc1" : "=v"(r) : "v"(off), "s"(c.ws) : "memory"); return r;
}
template <int N> DI void check_flag(const Ctx& c, v2u& f, u32 off, u64 tag) {
  asm volatile("s_waitcnt vmcnt(%1)" : "+v"(f) : "i"(N) : "memory");
  const u32 f0 = f.x, f1 = f.y;
  if (!__all((((u64)f1 << 32) | f0) == tag)) poll_at(c, off, tag);
}
DI u32 tileA(const Ctx& c, int i) { const u32 ga = (c.rot2 + (u32)i) & 15u, j = ga >> 1; return umin(6u * j, 5u * j + 3u) + 2u * (ga & 1u); }
constexpr u32 tileB0(int i) { return i < 19 ? 6u * (u32)(i - 16) + 4u : 10u * (u32)(i - 19) + 22u; }

template <int I> DI void issue_w2(const Ctx& c) {
  if constexpr (I < 16) glds<slot2(I), 0>(c.w2, c.w2off + (tileA(c, I) << 15), c.wb1k);
  else if constexpr (I < 19) glds<slot2(I), 0>(c.w2, c.w2off + (tileB0(I) << 15), c.wb1k);
  else if constexpr (I < 21) glds<slot2(I), 0>(c.w2, c.w2off + (tileB0(I) << 15) + 4u * c.whi, c.wb1k);
  else if constexpr (I == 21) glds<slot2(I), 0>(c.w2, c.w2off + (tileB0(I) << 15) - c.whi, c.wb1k);
  else glds<slot2(I), 0>(c.wo, c.w2off + (((c.rot2 + (u32)I) & 7u) << 16), c.wb1k);
}
template <int I> DI void load_w2(const Ctx& c, v4u (&wr)[12]) {
  const u8* base = I < 22 ? c.w2 : c.wo;
  const u32 off = I == 18 ? c.w2off + (tileB0(I) << 15) : I < 21 ? c.w2off + (tileB0(I) << 15) + 4u * c.whi : I == 21 ? c.w2off + (tileB0(I < 22 ? I : 21) << 15) - c.whi
                                                                                                          : c.w2off + (((c.rot2 + (u32)I) & 7u) << 16);
  asm volatile("global_load_dwordx4 %0, %1, %2" : "=v"(wr[I - 18]) : "v"(off), "s"(base) : "memory");
}
template <int I0, int N> DI void issue_w2s(const Ctx& c) {
  if constexpr (N > 0) { issue_w2<I0>(c); issue_w2s<I0 + 1, N - 1>(c); }
}
#define ROTB(S) ((c.rot1 + (u32)(S)) & 7u)
template <int ROLE, int S> DI void issue_wB(const Ctx& c) {
  constexpr u32 RS = 64u * (ROLE == 0 ? 3072u : 2752u);
  glds<slotB(S), (ROLE == 0 ? 0 : 256)>(c.wb, c.woffB + ROTB(S) * RS, c.wb1k);
}
template <int ROLE, int U> DI void issue_unit(Ctx& c) {
  if constexpr (U >= 39) {
    issue_w2<U - 39>(c);
  } else if constexpr (U >= 32) issue_wB<ROLE, U - 31>(c);
  else {
    constexpr int S = U / 4, J = U % 4;
    constexpr u32 RS = 64u * (ROLE == 0 ? 3072u : 2752u);
    if constexpr (J == 0) {
      const u32 ks = (c.rot1 + (u32)S) & 7u;
      c.vx = c.xoff + ks * 128u;
      c.vw = c.woff + ks * RS;
    }
    if constexpr (J < 2) glds<ring(U), 0, (U < NTU ? NTX : 0)>(J == 0 ? c.xb0 : c.xb1, c.vx, c.wb1k);
    else glds<ring(U), (ROLE == 0 ? (J - 1) * 1024 : (J - 2) * 128), (U < NTU ? NTW : 0)>(c.wb, c.vw, c.wb1k);
  }
}
template <int ROLE, int U0, int N> DI void issue_units(Ctx& c) {
  if constexpr (N > 0) { issue_unit<ROLE, U0>(c); issue_units<ROLE, U0 + 1, N - 1>(c); }
}
template <int CTRL> DI float dpp_add(float v) {
  return v + __builtin_bit_cast(float, __builtin_amdgcn_update_dpp(0, __builtin_bit_cast(int, v), CTRL, 0xF, 0xF, true));
}
template <int CTRL> DI float dpp_add2(float keep, float give) {
  return keep + __builtin_bit_cast(float, __builtin_amdgcn_update_dpp(0, __builtin_bit_cast(int, give), CTRL, 0xF, 0xF, true));
}
DI float wave_sum(float v) {
  v = dpp_add<0xB1>(v); v = dpp_add<0x4E>(v); v = dpp_add<0x141>(v); v = dpp_add<0x140>(v);
  const int i = __builtin_bit_cast(int, v);
  return (__builtin_bit_cast(float, __builtin_amdgcn_readlane(i, 0)) + __builtin_bit_cast(float, __builtin_amdgcn_readlane(i, 16))) +
         (__builtin_bit_cast(float, __builtin_amdgcn_readlane(i, 32)) + __builtin_bit_cast(float, __builtin_amdgcn_readlane(i, 48)));
}

struct G1 {
  u32 xa[3][4], wa[3][4], xw;
  v4u x[8][4];
  v4u wf[3][2];
  f16v acc[3];
  v4u st, csv;
  v2u f1, f2;
};
DI u32 wad(const G1& g, int i, u32 a) { return g.wa[a >> 16][i] + (a & 0xFFFFu); }
template <int S, int I, int P> DI void rd(G1& g) {
  if constexpr (S < 8) {
    constexpr u32 X0 = ring(4 * S), X1 = ring(4 * S + 1);
    if constexpr (P == 0) {
      if constexpr (X1 == X0 + 8192u) g.x[S][I] = lds128(g.xa[X0 >> 16][I] + (X0 & 0xFFFFu));
      else g.x[S][I] = lds128(g.xa[0][I] + g.xw);
    }
    g.wf[(4 * S + I) % 3][P] = tr_frag<128>(wad(g, I, ring(4 * S + 2 + P)));
  }
}
template <int S, int I, int P> DI void mm(G1& g) {
  g.acc[P] = mfma32(g.wf[(4 * S + I) % 3][P], g.x[S][I], g.acc[P]);
  SCHED();
}
constexpr int PROG = 2;
constexpr int P1U[8][9] = {{12, 13, 14, -1, 15, 16, 17, -1, 18}, {19, -1, 20, -1, -1, 21, -1, 22, -1}, {23, -1, 24, -1, -1, 25, -1, 26, -1}, {27, -1, 28, -1, -1, 29, -1, 30, -1}, {31, -1, 32, -1, -1, -1, 33, -1, -1}, {34, -1, 35, -1, -1, -1, 36, -1, -1}, {37, -1, 38, -1, -1, -1, 39, -1, -1}, {40, -1, 41, -1, -1, 42, -1, -1, -1}};
constexpr int P1W[2][8] = {{9, 9, 10, 10, 9, 9, 9, 3}, {9, 10, 10, 11, 10, 10, 11, 3}};
constexpr int P1CHK[2] = {5, 5}, P1CSV = 4;
template <int ROLE, int S, int G> DI void dma(Ctx& c) {
  if constexpr (P1U[S][G] >= 0) issue_unit<ROLE, (P1U[S][G] >= 0 ? P1U[S][G] : 0)>(c);
  SCHED();
}
#define FS_OFF (WS_FS + (u32)((c.t * 128 + c.wm * 32 + (c.lane & 31)) * 8))
#define FC_OFF (WS_FC + (u32)(((c.pj * 8 + ((c.lane >> 2) & 7)) * 4 + ((c.lane & 3) == 3 ? 0 : (c.lane & 3))) * 8))
template <int ROLE, int S> DI void stage(Ctx& c, G1& g) {
  mm<S, 0, 0>(g); rd<S, 2, 0>(g); dma<ROLE, S, 0>(c);
  mm<S, 0, 1>(g); rd<S, 2, 1>(g);
  if constexpr (S == 6) {
    const u64 tag = read_tag();
    if constexpr (ROLE == 1) asm volatile("s_waitcnt vmcnt(%1)" : "+v"(g.f2) : "i"(P1CHK[1]) : "memory");
    check_flag<P1CHK[ROLE]>(c, g.f1, FS_OFF, tag);
    if constexpr (ROLE == 1) check_flag<P1CHK[1]>(c, g.f2, FC_OFF, tag);
    g.st = gload_sc1(c.ws, WS_ST + (u32)((c.t * 128 + c.wm * 32 + (c.lane & 31)) * 16));
    if constexpr (ROLE == 1) g.csv = gload_sc1(c.ws, WS_CS + (u32)(((c.pj * 8 + (c.lane & 7)) * 192 + ((c.w < 6 ? c.w : 5) * 8 + (c.lane >> 3)) * 4) * 4));
  }
  dma<ROLE, S, 1>(c);
  if constexpr (ROLE == 1 && S == 0) {
    const u32 a = L_CS + (u32)((c.wn * 32 + (c.lane & 31)) * 32 + (c.wm * 2 + (c.lane >> 5)) * 4);
    const v4u wB = tr_frag<128>(wad(g, 0, slotB(0)));
    sts32(a, sum8(g.wf[0][0])); sts32(a + 2048, sum8(g.wf[0][1])); sts32(a + 4096, sum8(wB));
    SCHED();
  }
  mm<S, 1, 0>(g); rd<S, 3, 0>(g); dma<ROLE, S, 2>(c);
  mm<S, 1, 1>(g); rd<S, 3, 1>(g); dma<ROLE, S, 3>(c);
  if constexpr (ROLE == 1 && S == 7) {
    asm volatile("s_waitcnt vmcnt(%2)" : "+v"(g.csv), "+v"(g.st) : "i"(P1CSV) : "memory");
    const u32 c0 = g.csv.x, c1 = g.csv.y, c2 = g.csv.z, c3 = g.csv.w;
    float v0 = __builtin_bit_cast(float, c0), v1 = __builtin_bit_cast(float, c1), v2 = __builtin_bit_cast(float, c2), v3 = __builtin_bit_cast(float, c3);
    const u32 col = (u32)((c.w * 8 + (c.lane >> 3)) * 4 + (c.lane & 3));
    const u32 b16 = *(const LAS u16*)(size_t)(L_T3 + col * 2u);
    const bool o1 = c.lane & 1, o2 = c.lane & 2;
    float p0 = dpp_add2<0xB1>(o1 ? v1 : v0, o1 ? v0 : v1), p1 = dpp_add2<0xB1>(o1 ? v3 : v2, o1 ? v2 : v3);
    float tot = dpp_add2<0x4E>(o2 ? p1 : p0, o2 ? p0 : p1);
    tot += __builtin_bit_cast(float, __builtin_amdgcn_update_dpp(0, __builtin_bit_cast(int, tot), 0x114, 0xF, 0xA, false) |
                                     __builtin_amdgcn_update_dpp(0, __builtin_bit_cast(int, tot), 0x104, 0xF, 0x5, false));
    const u32 hi = pk2(tot, 0.f) & 0xFFFFu, lo = pk2(tot - bf2f(hi), 0.f) & 0xFFFFu;
    sts128(c.w < 6 ? L_CB + col * 16u : L_END + 256u, v4u{hi | (lo << 16), hi | (b16 << 16), b16, 0u});
    SCHED();
  }
  mm<S, 2, 0>(g); dma<ROLE, S, 4>(c);
  if constexpr (S == 7) asm volatile("s_waitcnt vmcnt(%1) lgkmcnt(0)\n\ts_barrier" : "+v"(g.st) : "i"(P1W[ROLE][7]) : "memory");
  else wait_bar<P1W[ROLE][S]>();
  rd<S + 1, 0, 0>(g);
  if constexpr (ROLE == 1 && S == 0) {
    const int tid = c.w * 64 + c.lane, i = tid & 31;
    const u32 la = L_CS + (u32)(tid < 192 ? tid : 0) * 32u;
    const f4 a = ldsf4(la), bq = ldsf4(la + 16);
    const int f = (tid & ~31) + (i & 16) + 8 * ((i >> 2) & 1) + 4 * ((i >> 3) & 1) + (i & 3);
    const u32 off = (tid < 192 && c.t < 8) ? WS_CS + (u32)(((c.pj * 8 + c.t) * 192 + f) * 4) : OOB;
    __builtin_amdgcn_raw_buffer_store_b32(__builtin_bit_cast(u32, ((a[0] + a[1]) + (a[2] + a[3])) + ((bq[0] + bq[1]) + (bq[2] + bq[3]))), c.rs, (int)off, 0, AUX_SC1);
  }
  if constexpr (S == 1) raise_wave_flag(c, WS_FS + (u32)((c.t * 128 + ROLE * 64 + c.pj * 8 + c.w) * 8), read_tag());
  if constexpr (S == 2 && ROLE == 1) raise_wave_flag(c, (c.w < 3 && c.t < 8) ? WS_FC + (u32)(((c.pj * 8 + c.t) * 4 + c.w) * 8) : OOB, read_tag());
  if constexpr (S == 1) {
    if (c.w == 7 && c.lane < 59) {
      const u32 line = blockIdx.x * 59u + (u32)c.lane;
      const u8* pa = line < 11008u ? c.w2 + (size_t)line * 128 : c.wo + (size_t)(line - 11008u) * 128;
      asm volatile("s_mov_b32 m0, %1\n\ts_nop 0\n\tglobal_load_lds_dword %0, off" :: "v"(pa), "i"(L_END + 256) : "memory");
    }
  }
  if constexpr (S == 4) { g.f1 = fetch_flag(c, FS_OFF); if constexpr (ROLE == 1) g.f2 = fetch_flag(c, FC_OFF); }
  dma<ROLE, S, 5>(c);
  mm<S, 2, 1>(g); rd<S + 1, 0, 1>(g); dma<ROLE, S, 6>(c);
  mm<S, 3, 0>(g); rd<S + 1, 1, 0>(g); dma<ROLE, S, 7>(c);
  mm<S, 3, 1>(g); rd<S + 1, 1, 1>(g); dma<ROLE, S, 8>(c);
  asm volatile("" : "+v"(g.acc[0]), "+v"(g.acc[1]));
  if constexpr (S < 7) stage<ROLE, S + 1>(c, g);
}
template <int N> DI v4u& bbuf(G1& g) { return g.wf[(N & 3) == 3 ? 0 : (N & 3)][(N & 3) == 3 ? 1 : 0]; }
template <int N> DI void rdB(G1& g) { if constexpr (N < 32) bbuf<N>(g) = tr_frag<128>(wad(g, N & 3, slotB(N >> 2))); }
template <int N0, int N> DI void passB(G1& g) {
  if constexpr (N > 0) {
    g.acc[2] = mfma32(bbuf<N0>(g), g.x[N0 >> 2][N0 & 3], g.acc[2]); OPAQUE(g.acc[2]); SCHED();
    rdB<N0 + 3>(g);
    SCHED();
    passB<N0 + 1, N - 1>(g);
  }
}

template <bool KEEP, class HOOK> DI float hedgehog(const f16v& p, v4u (&o)[4], HOOK hook) {
  constexpr float L2E = 1.4426950408889634f;
  float mx = p[0], mn = p[0];
#pragma unroll
  for (int i = 1; i < 16; ++i) { mx = fmaxf(mx, p[i]); mn = fminf(mn, p[i]); }
  const f2 kp = {L2E, L2E}, kn = {-L2E, -L2E}, cp = {-mx * L2E, -mx * L2E}, cn = {mn * L2E, mn * L2E};
  f2 ep[8], en[8];
#pragma unroll
  for (int i = 0; i < 8; ++i) {
    const f2 v = {p[2 * i], p[2 * i + 1]};
    const f2 a = __builtin_elementwise_fma(v, kp, cp), b = __builtin_elementwise_fma(v, kn, cn);
    ep[i] = f2{__builtin_amdgcn_exp2f(a[0]), __builtin_amdgcn_exp2f(a[1])};
    en[i] = f2{__builtin_amdgcn_exp2f(b[0]), __builtin_amdgcn_exp2f(b[1])};
    hook(i, en[i]);
  }
  const f2 s1 = ((ep[0] + ep[1]) + (ep[2] + ep[3])) + ((ep[4] + ep[5]) + (ep[6] + ep[7]));
  const f2 s2 = ((en[0] + en[1]) + (en[2] + en[3])) + ((en[4] + en[5]) + (en[6] + en[7]));
  const float sp = s1[0] + s1[1], rn0 = __builtin_amdgcn_rcpf(s2[0] + s2[1]);
  const float rp = KEEP ? 1.f : __builtin_amdgcn_rcpf(sp), rn = KEEP ? sp * rn0 : rn0;
  u32 a[8], b[8];
#pragma unroll
  for (int i = 0; i < 8; ++i) {
    const f2 x = KEEP ? ep[i] : ep[i] * f2{rp, rp}, y = en[i] * f2{rn, rn};
    a[i] = pk2(x[0], x[1]); b[i] = pk2(y[0], y[1]);
  }
  o[0] = v4u{a[0], a[1], a[2], a[3]}; o[1] = v4u{a[4], a[5], a[6], a[7]};
  o[2] = v4u{b[0], b[1], b[2], b[3]}; o[3] = v4u{b[4], b[5], b[6], b[7]};
  return sp;
}
DI v4u pack8(const f16v& a, int s) {
  return v4u{pk2(a[8 * s], a[8 * s + 1]), pk2(a[8 * s + 2], a[8 * s + 3]), pk2(a[8 * s + 4], a[8 * s + 5]), pk2(a[8 * s + 6], a[8 * s + 7])};
}
DI void ln_fold(const Ctx& c, f16v& acc, int i, v4u lnb) {
  const int r = c.lane & 31;
  const int f = (r & 16) + 8 * ((r >> 2) & 1) + 4 * ((r >> 3) & 1) + (r & 3);
  acc = mfma32(lds128(c.lane < 32 ? L_CB + (u32)((i * 64 + c.wn * 32 + f) * 16) : L_Z0), lnb, acc);
}
DI void gelu_half(const Ctx& c, const f16v& acc, int s, float rstd, float io, u32 off) {
  constexpr float C1 = -2.302208198144325f, C2 = -2.302208198144325f * 0.044715f;
  u32 o[4];
#pragma unroll
  for (int e = 0; e < 4; ++e) {
    const f2 v = f2{acc[8 * s + 2 * e], acc[8 * s + 2 * e + 1]} * f2{rstd, rstd};
    const f2 a = v * __builtin_elementwise_fma(v * v, f2{C2, C2}, f2{C1, C1});
    const f2 d = __builtin_elementwise_fma(f2{__builtin_amdgcn_exp2f(a[0]), __builtin_amdgcn_exp2f(a[1])}, f2{io, io}, f2{io, io});
    const f2 y = v * f2{__builtin_amdgcn_rcpf(d[0]), __builtin_amdgcn_rcpf(d[1])};
    o[e] = pk2(y[0], y[1]);
  }
  __builtin_amdgcn_raw_buffer_store_b128(v4u{o[0], o[1], o[2], o[3]}, c.rs, (int)off, 0, AUX_SC1);
}

template <int ROLE> DI void passA(Ctx& c, G1& g, const u8* tb, u32 tdst, int tlim, const u8* xrow, const u8* gw) {
  const int lane = c.lane, wm = c.wm, wn = c.wn;
  v4u xv, gv;
  asm volatile("global_load_dwordx4 %0, %2, %3\n\tglobal_load_dwordx4 %1, %2, %4" : "=&v"(xv), "=&v"(gv) : "v"(lane * 16), "s"(xrow), "s"(gw) : "memory");
  issue_units<ROLE, 0, 4>(c);
  issue_wB<ROLE, 0>(c);
  if (c.w < 4) glds_sc1(tb, (u32)(lane < tlim ? lane : tlim) * 16u, tdst);
  __builtin_amdgcn_s_barrier();
  issue_units<ROLE, 4, 4>(c);
  SCHED();
  const int h = lane >> 5, blk = (lane >> 4) & 1, q = (lane & 15) >> 2, pp = lane & 3;
  const int rot = (wm + 2 * wn) & 3;
  const int row = (wm & 1) * 32 + (lane & 31);
  const int col0 = ROLE == 0 ? 16 * (pp & 1) + 8 * blk + 4 * (pp >> 1) : 16 * blk + 8 * (pp & 1) + 4 * (pp >> 1);
  const u32 wl = (u32)((8 * h + q) * 128 + (((4 * wn + (col0 >> 3)) ^ (((q >> 1) & 1) << 2)) * 16) + 8 * ((col0 >> 2) & 1));
#pragma unroll
  for (int i = 0; i < 4; ++i) {
    const int kk = (i + rot) & 3;
    g.xa[0][i] = (u32)((wm >> 1) * 8192 + row * 128 + (((2 * kk + h) ^ ((row >> 1) & 7)) * 16));
    g.wa[0][i] = wl + (u32)kk * 2048u;
#pragma unroll
    for (int k = 1; k < 3; ++k) {
      g.xa[k][i] = g.xa[0][i] + (u32)k * 65536u; g.wa[k][i] = g.wa[0][i] + (u32)k * 65536u;
      asm volatile("" : "+v"(g.xa[k][i]), "+v"(g.wa[k][i]));
    }
  }
  g.xw = wm >= 2 ? ring(17) - 8192u : ring(16);
#pragma unroll
  for (int i = 0; i < 2; ++i)
#pragma unroll
    for (int e = 0; e < 16; ++e) g.acc[i][e] = 0.f;
  asm volatile("s_waitcnt vmcnt(9)" : "+v"(xv), "+v"(gv) :: "memory");
  issue_units<ROLE, 8, 2>(c);
  {
    float v[8], sm = 0.f, gd = 0.f;
#pragma unroll
    for (int e = 0; e < 4; ++e) {
      const u32 a = xv[e], bq = gv[e];
      v[2 * e] = bf2f(a & 0xFFFFu); v[2 * e + 1] = bfhi(a);
      sm += v[2 * e] + v[2 * e + 1];
      gd = __builtin_fmaf(v[2 * e], bf2f(bq & 0xFFFFu), gd); gd = __builtin_fmaf(v[2 * e + 1], bfhi(bq), gd);
    }
    const float mean = wave_sum(sm) * (1.f / DM);
    float sq = 0.f;
#pragma unroll
    for (int e = 0; e < 8; ++e) { const float d = v[e] - mean; sq = __builtin_fmaf(d, d, sq); }
    const float sd = __builtin_amdgcn_sqrtf(wave_sum(sq) * (1.f / DM) + 1e-5f);
    const float eg = wave_sum(gd) * 1.4426950408889634f;
    const f4 o = {-mean, sd, 1.f + __builtin_amdgcn_exp2f(fminf(eg, 100.f)), __builtin_amdgcn_rcpf(1.f + __builtin_amdgcn_exp2f(-eg))};
    __builtin_amdgcn_raw_buffer_store_b128(__builtin_bit_cast(v4u, o), c.rs, (int)(lane == 0 ? WS_ST + (u32)((c.t * 128 + ROLE * 64 + c.pj * 8 + c.w) * 16) : OOB), 0, AUX_SC1);
  }
  if (c.w == 5 && lane < 32) sts128(L_Z0 + (u32)lane * 16u, v4u{0u, 0u, 0u, 0u});
  if constexpr (PROG >= 2) {
    asm volatile("s_waitcnt vmcnt(%0)" :: "i"(7 + PROG) : "memory"); issue_unit<ROLE, 10>(c);
    asm volatile("s_waitcnt vmcnt(%0)" :: "i"(7 + PROG) : "memory"); issue_unit<ROLE, 11>(c);
  }
  if constexpr (PROG >= 4) {
    asm volatile("s_waitcnt vmcnt(11)" ::: "memory"); issue_unit<ROLE, 12>(c);
    asm volatile("s_waitcnt vmcnt(11)" ::: "memory"); issue_unit<ROLE, 13>(c);
  }
  wait_bar<7 + PROG>();
  rd<0, 0, 0>(g); rd<0, 0, 1>(g); rd<0, 1, 0>(g); rd<0, 1, 1>(g); SCHED();
  stage<ROLE, 0>(c, g);
}

#define STAMP(i) do { } while (0)
#define TS_PARAM
#define TS_ARG

DI void advance_epoch(const Ctx& c, u64 tag) {
  if (blockIdx.x == 0 && c.w == 7) {
#pragma unroll 1
    for (u32 k = 0; k < 4; ++k) poll_at(c, WS_FS + (k * 64u + (u32)c.lane) * 64u, tag);
    if (c.lane == 0) __hip_atomic_store((GAS u64*)(c.ws + WS_EPOCH), tag, RLX_AGENT);
  }
}

template <int I0, int I1> DI void p2_early(const Ctx& c, int mt, v4u (&af)[30]) {
  const u8* ab = c.ws + WS_A + (size_t)(mt * 2 + (c.w & 1)) * (NKS * 1024u);
  const u32 al = (u32)c.lane * 16u + (u32)(c.w >> 1) * 1024u;
#pragma unroll
  for (int i = I0; i < I1; ++i) af[i] = gload_sc1(ab, al + (tileA(c, i) << 11));
}
template <int ROLE> DI void phase2(const Ctx& c, int mt, int nt, u16* out, v4u (&af)[30], v4u (&wr)[12] TS_PARAM) {
  const int lane = c.lane, h = lane >> 5, tok = lane & 31;
  const u64 tag = read_tag();
  const int wm2 = c.w & 1, wk = c.w >> 1;
  const u8* ab = c.ws + WS_A + (size_t)(mt * 2 + wm2) * (NKS * 1024u);
  const u32 al = (u32)lane * 16u + (u32)wk * 1024u;
  const u32 a5 = al + (wk >= 2 ? 4u * 2048u : 0u), a1 = al - (wk >= 2 ? 2048u : 0u);
  const int ftok = c.w * 8 + (lane >> 3), fch = lane & 7;
  v4u wf[2][2], st2;
  v2u fb, fo;
  const u32 wws = c.wb1k + (u32)lane * 16u;
  const u32 fooff = WS_F2 + (u32)(((mt * 2 + wm2) * 32 + (lane & 15)) * 8), fboff = fooff + 128u;
  f16v o2[2];
  u32 wh[3][2], wo[3][2];
#define LDA(i) af[i] = gload_sc1(ab, (i) < 16 ? al + (tileA(c, (i)) << 11) : (i) < 22 ? ((i) < 19 ? al : (i) < 21 ? a5 : a1) + (tileB0((i) < 22 && (i) >= 16 ? (i) : 16) << 11) \
                                              : al + 86u * 1024u + (((c.rot2 + (u32)(i)) & 7u) << 12))
#define RDW1(i, k) wf[(i) & 1][k] = tr_frag<128>(((i) < 22 ? wh[slot2(i) >> 16][k] : wo[slot2(i) >> 16][k]) + (slot2(i) & 0xFFFFu))
#define RDW(i) RDW1(i, 0); RDW1(i, 1)
#define WRW(i, N) wait_for<N>(wr[(i) - 18]); sts128(wws + slot2(i), wr[(i) - 18])
#define STEP(i, N, NX) wait_for<N>(af[i]); \
    if ((i) != 21 || wk < 2) o2[0] = mfma32(wf[(i) & 1][0], af[i], o2[0]); SCHED(); if ((NX) >= 0) { RDW1((NX) >= 0 ? (NX) : 0, 0); } SCHED(); \
    if ((i) != 21 || wk < 2) o2[1] = mfma32(wf[(i) & 1][1], af[i], o2[1]); SCHED(); if ((NX) >= 0) { RDW1((NX) >= 0 ? (NX) : 0, 1); } SCHED()
#define P2_SETUP() do { \
    _Pragma("unroll") for (int i = 0; i < 2; ++i) _Pragma("unroll") for (int e = 0; e < 16; ++e) o2[i][e] = 0.f; \
    const int blk = (lane >> 4) & 1, q = (lane & 15) >> 2, pp = lane & 3; \
    const int col0 = 16 * (pp & 1) + 8 * blk + 4 * (pp >> 1); \
    const u32 cb = (u32)(((col0 >> 3) ^ (((q >> 1) & 1) << 2)) * 16 + 8 * ((col0 >> 2) & 1)); \
    const u32 rh = (u32)((16 * wk + 8 * h + q) * 128), ro = (u32)((32 * (wk >> 1) + 16 * h + 8 * (wk & 1) + q) * 128); \
    _Pragma("unroll") for (int k = 0; k < 3; ++k) { \
      wh[k][0] = rh + cb + (u32)k * 65536u; wh[k][1] = rh + (cb ^ 64u) + (u32)k * 65536u; wo[k][0] = ro + cb + (u32)k * 65536u; wo[k][1] = ro + (cb ^ 64u) + (u32)k * 65536u; \
      asm volatile("" : "+v"(wh[k][0]), "+v"(wh[k][1]), "+v"(wo[k][0]), "+v"(wo[k][1])); } } while (0)
  if constexpr (ROLE == 1) {
  P2_SETUP();
  BAR_L();
  LDA(6); LDA(7); RDW(0); STEP(0, 9, 1);
  STEP(1, 8, 2);
  STEP(2, 7, 3);
  asm volatile("s_waitcnt vmcnt(2)" ::: "memory"); raise_wave_flag(c, WS_F2 + (u32)((((c.t * 4 + c.wm) * 16 + ROLE * 8 + c.pj) * 2 + c.wn) * 8), tag); STAMP(6);
  LDA(8); LDA(9); LDA(10); LDA(11); STEP(3, 11, 4);
  LDA(12); LDA(13); issue_w2<14>(c); issue_w2<15>(c); STEP(4, 14, 5);
  LDA(14); LDA(15); issue_w2<16>(c); issue_w2<17>(c); STEP(5, 17, 6);
  BAR_L();
  fb = fetch_flag(c, fboff); issue_w2<23>(c); WRW(18, 29); STEP(6, 16, 7);
  WRW(19, 28); STEP(7, 15, 8);
  st2 = gload_sc1(c.ws, WS_ST + (u32)((mt * 64 + ftok) * 16)); WRW(20, 28); STEP(8, 14, 9);
  check_flag<2>(c, fb, fboff, tag);
  LDA(16); LDA(17); LDA(18); WRW(21, 30); STEP(9, 16, 10);
  LDA(19); LDA(20); LDA(21); WRW(22, 32); STEP(10, 18, 11);
  STEP(11, 17, 12);
  wait_bar<9>(); STAMP(7);
  issue_w2<24>(c); issue_w2<25>(c); STEP(12, 18, 13);
  issue_w2<26>(c); issue_w2<27>(c); STEP(13, 19, 14);
  issue_w2<28>(c); issue_w2<29>(c); STEP(14, 18, 15);
  STEP(15, 17, 16);
  fo = fetch_flag(c, fooff); STEP(16, 12, 17);
  STEP(17, 11, 18);
  wait_bar<14>(); STAMP(8);
  STEP(18, 10, 19);
  STEP(19, 9, 20);
  STEP(20, 8, 21);
  STEP(21, 7, 22);
  check_flag<0>(c, fo, fooff, tag);
  LDA(22); LDA(23); LDA(24); LDA(25); LDA(26); LDA(27); LDA(28); LDA(29); advance_epoch(c, tag);
  wait_bar<9>();
  STEP(22, 7, 23);
  STEP(23, 6, 24);
  STEP(24, 5, 25);
  STEP(25, 4, 26);
  STEP(26, 3, 27);
  STEP(27, 2, 28);
  STEP(28, 1, 29);
  STEP(29, 0, -1);

  } else {
  __builtin_amdgcn_s_barrier();
  LDA(6); LDA(7); P2_SETUP();
  RDW(0); STEP(0, 9, 1);
  STEP(1, 8, 2);
  STEP(2, 7, 3);
  asm volatile("s_waitcnt vmcnt(2)" ::: "memory"); raise_wave_flag(c, WS_F2 + (u32)((((c.t * 4 + c.wm) * 16 + ROLE * 8 + c.pj) * 2 + c.wn) * 8), tag); STAMP(6);
  LDA(8); LDA(9); LDA(10); LDA(11); LDA(12); LDA(13); STEP(3, 13, 4);
  LDA(14); LDA(15); fb = fetch_flag(c, fboff); STEP(4, 15, 5);
  STEP(5, 14, 6);
  BAR_L();
  issue_w2<22>(c); issue_w2<23>(c); WRW(18, 25); STEP(6, 13, 7);
  WRW(19, 24); STEP(7, 12, 8);
  check_flag<2>(c, fb, fboff, tag);
  LDA(16); LDA(17); LDA(18); WRW(20, 26); STEP(8, 13, 9);
  LDA(19); LDA(20); LDA(21); WRW(21, 28); STEP(9, 15, 10);
  fo = fetch_flag(c, fooff); st2 = gload_sc1(c.ws, WS_ST + (u32)((mt * 64 + ftok) * 16)); STEP(10, 16, 11);
  STEP(11, 15, 12);
  BAR_L(); STAMP(7);
  issue_w2<24>(c); issue_w2<25>(c); STEP(12, 16, 13);
  check_flag<3>(c, fo, fooff, tag);
  LDA(22); LDA(23); issue_w2<26>(c); issue_w2<27>(c); STEP(13, 19, 14);
  LDA(24); LDA(25); issue_w2<28>(c); issue_w2<29>(c); STEP(14, 22, 15);
  LDA(26); LDA(27); STEP(15, 23, 16);
  LDA(28); LDA(29); STEP(16, 21, 17);
  STEP(17, 20, 18);
  wait_bar<22>(); STAMP(8);
  STEP(18, 19, 19);
  STEP(19, 18, 20);
  STEP(20, 17, 21);
  advance_epoch(c, tag);
  STEP(21, 16, 22);
  STEP(22, 11, 23);
  STEP(23, 10, -1);
  wait_bar<4>();
  RDW(24); STEP(24, 7, 25);
  STEP(25, 6, 26);
  STEP(26, 3, 27);
  STEP(27, 2, 28);
  STEP(28, 1, 29);
  STEP(29, 0, -1);

  }
  asm volatile("" : "+v"(o2[0]), "+v"(o2[1]), "+v"(st2));
  STAMP(9);
  BAR_L();
#pragma unroll
  for (int cc = 0; cc < 2; ++cc)
#pragma unroll
    for (int qd = 0; qd < 4; ++qd) {
      const u32 sl = (u32)((tok + 4 * (2 * h + (qd >> 1))) & 31);
      stsf4(RB + (u32)(((c.w * 2 + cc) * 4 + qd) * 1024 + h * 512) + sl * 16u, f4{o2[cc][4 * qd], o2[cc][4 * qd + 1], o2[cc][4 * qd + 2], o2[cc][4 * qd + 3]});
    }
  BAR_L();
  {
    const int cc = fch >> 2, hh = (fch >> 1) & 1, q0 = 2 * (fch & 1);
    const u32 sl = (u32)(((ftok & 31) + 4 * (fch & 3)) & 31);
    const u32 rb = RB + (u32)((((ftok >> 5) * 2 + cc) * 4 + q0) * 1024 + hh * 512) + sl * 16u;
    f4 s0 = ldsf4(rb), s1 = ldsf4(rb + 1024);
#pragma unroll
    for (int k = 1; k < 4; ++k) { s0 += ldsf4(rb + (u32)k * 16384u); s1 += ldsf4(rb + (u32)k * 16384u + 1024u); }
    const v4u bb = lds128(L_B2 + (u32)(fch * 16));
    const u32 b0 = bb.x, b1_ = bb.y, b2_ = bb.z, b3 = bb.w, ob = st2.z;
    const float omg2 = __builtin_amdgcn_rcpf(__builtin_bit_cast(float, ob));
    const v4u o = {pk2(__builtin_fmaf(omg2, bf2f(b0 & 0xFFFFu), s0[0]), __builtin_fmaf(omg2, bfhi(b0), s0[1])),
                   pk2(__builtin_fmaf(omg2, bf2f(b1_ & 0xFFFFu), s0[2]), __builtin_fmaf(omg2, bfhi(b1_), s0[3])),
                   pk2(__builtin_fmaf(omg2, bf2f(b2_ & 0xFFFFu), s1[0]), __builtin_fmaf(omg2, bfhi(b2_), s1[1])),
                   pk2(__builtin_fmaf(omg2, bf2f(b3 & 0xFFFFu), s1[2]), __builtin_fmaf(omg2, bfhi(b3), s1[3]))};
    *(GAS v4u*)(out + (size_t)(mt * 64 + ftok) * DM + nt * 64 + fch * 8) = o;
  }
}

__global__ void __launch_bounds__(512, 2)
fused_kernel(const u16* __restrict__ x, const u16* __restrict__ wqkv, const u16* __restrict__ wphi, const u16* __restrict__ wo,
             const u16* __restrict__ w1, const u16* __restrict__ b1, const u16* __restrict__ w2, const u16* __restrict__ b2,
             const u16* __restrict__ gw, u16* __restrict__ out, u8* __restrict__ ws) {
  Ctx c;
  const int tid = threadIdx.x, lane = tid & 63;
  c.lane = lane; c.ws = ws;
  c.w = __builtin_amdgcn_readfirstlane(tid >> 6);
  STAMP(0);
  c.wb1k = (u32)c.w * 1024u;
  const int wm = c.w & 3, wn = c.w >> 2;
  const int b = blockIdx.x, xg = b & 7, r = b >> 3;
  const int t = (xg & 1) * 8 + (r >> 2);
  const bool is_att = (r & 2) == 0;
  const int pj = 2 * (xg >> 1) + (r & 1);
  const int tile0 = pj < 3 ? 6 * pj : 3 + 5 * pj;
  const bool six = pj < 3;
  const int mt = xg * 4 + (r >> 3), nt = r & 7;
  c.t = t; c.pj = pj; c.wm = wm; c.wn = wn;
  const int r8 = lane >> 3, pos = lane & 7;
  const int xr = (tid >> 6) * 8 + r8;
  const u32 ch = (u32)((pos ^ (((r8 >> 1) & 1) << 2)) * 16);
  c.xb0 = (const u8*)x + (size_t)t * 131072; c.xb1 = c.xb0 + 65536;
  c.xoff = (u32)(xr * 1024 + ((pos ^ ((xr >> 1) & 7)) * 16));
  c.rot1 = (u32)(t + pj + (r & 2) * 2); OPAQUE(c.rot1);
  const u8* xrow = c.xb0 + (size_t)(((r & 2) * 32 + pj * 8 + c.w) * 1024);
  c.rs = __builtin_amdgcn_make_buffer_rsrc(ws, 0, WS_END, 0x00020000);
  const int h = lane >> 5, tok = lane & 31;
  const int ltok = wm * 32 + tok;
  const u32 abase = WS_A + (u32)(t * 4 + wm) * (NKS * 1024u) + (u32)lane * 16u;
  c.rot2 = (u32)(nt * 2 + (mt & 1) + (mt >> 1) * 5); OPAQUE(c.rot2);
  c.w2off = (u32)(xr * 1024 + nt * 128) + ch;
  c.whi = (u32)(tid >> 8) << 15;
  c.w2 = (const u8*)w2; c.wo = (const u8*)wo;
  const u32 h1off = WS_FH1 + (u32)(((mt * 2 + (c.w & 1)) * 16 + (lane & 15)) * 8);
  const u8* tb = ws + WS_EPOCH; u32 tdst = L_EP; int tlim = 0;
  if (c.w == 1) { tb = (const u8*)(b2 + nt * 64); tdst = L_B2; tlim = 7; }
  G1 g;
  v4u af[30], wr[12];

  if (is_att) {
    c.wb = (const u8*)wqkv + pj * 128;
    c.woff = (u32)(xr * 3072) + ch; c.woffB = c.woff;
    if (c.w >= 2) { tb = (const u8*)(wphi + pj * 1024 + (c.w & 1) * 512); tdst = L_T3 + (u32)(c.w & 1) * 1024u; tlim = 63; }
    STAMP(11);
    passA<0>(c, g, tb, tdst, tlim, xrow, (const u8*)gw);
    STAMP(1);
    const int g4 = lane >> 4, q4 = (lane & 15) >> 2, p4 = lane & 3;
    const bool on = ((lane >> 2) & 1) == h;
    const v4u zero4 = {0u, 0u, 0u, 0u};
    auto phi_mm = [&](const f16v& a) __attribute__((always_inline)) {
      u32 pa = L_T3 + (u32)((2 * wn + (p4 & 1)) * 512 + q4 * 32 + (8 * (g4 & 1) + 4 * (p4 >> 1)) * 2);
      OPAQUE(pa);
      v4u a0 = tr_frag<32>(pa), a1 = tr_frag<32>(pa + 256);
      a0 = on ? a0 : zero4; a1 = on ? a1 : zero4;
      f16v z;
#pragma unroll
      for (int e = 0; e < 16; ++e) z[e] = 0.f;
      return mfma32(a1, pack8(a, 1), mfma32(a0, pack8(a, 0), z));
    };
    f16v zero16;
#pragma unroll
    for (int e = 0; e < 16; ++e) zero16[e] = 0.f;
    v4u ph[4];
    float spq;
    v2u fl1;
    const u64 tag = read_tag();
    const u32 kvflag = WS_FKV + (u32)(((pj * 8 + c.w) * 16 + t) * 8);
    {
      const f16v pkk = phi_mm(g.acc[0]);
      const u32 vb = slot(13) + (u32)((2 * wn + h) * 4096 + ltok * 32);
      sts128(vb, pack8(g.acc[1], 0)); sts128(vb + 16, pack8(g.acc[1], 1));
      hedgehog<false>(pkk, ph, [](int, f2&) {});
      rdB<0>(g); rdB<1>(g); rdB<2>(g);
      const u32 pkb = slot(9) + (u32)((2 * wn + h) * 8192 + ltok * 64);
      const u32 sw = (u32)(((ltok >> 1) & 3) ^ ((ltok >> 2) & 2)) * 16u;
      sts128(pkb + (0u ^ sw), ph[0]); sts128(pkb + (16u ^ sw), ph[1]); sts128(pkb + (32u ^ sw), ph[2]); sts128(pkb + (48u ^ sw), ph[3]);
#pragma unroll
      for (int e = 0; e < 16; ++e) g.acc[2][e] = 0.f;
      SCHED();
    }
    passB<0, 3>(g);
    BAR_L();
    STAMP(21);
    {
      f4 kvacc = {0.f, 0.f, 0.f, 0.f}, zacc = {0.f, 0.f, 0.f, 0.f};
      const v4u ones = {0x3F803F80u, 0x3F803F80u, 0x3F803F80u, 0x3F803F80u};
      const int hd = c.w >> 1, fh = c.w & 1;
      const int tk = 8 * g4 + q4;
      const int cq = 2 * fh + (p4 >> 1);
      const u32 pa0 = slot(9) + (u32)(hd * 8192 + tk * 64 + ((cq ^ ((tk >> 1) & 3) ^ ((tk >> 2) & 2)) * 16) + (p4 & 1) * 8);
      const u32 pa1 = slot(9) + (u32)(hd * 8192 + (tk + 4) * 64 + ((cq ^ (((tk + 4) >> 1) & 3) ^ (((tk + 4) >> 2) & 2)) * 16) + (p4 & 1) * 8);
      const u32 va = slot(13) + (u32)(hd * 4096 + tk * 32 + p4 * 8);
      v4u afr[4], bfr[4];
#pragma unroll
      for (int ks = 0; ks < 4; ++ks) {
        const v2u ua0 = tr64(pa0 + (u32)(ks * 2048)), ua1 = tr64(pa1 + (u32)(ks * 2048));
        afr[ks] = v4u{ua0.x, ua0.y, ua1.x, ua1.y};
        bfr[ks] = tr_frag<32>(va + (u32)(ks * 1024));
      }
      SCHED();
      passB<3, 2>(g);
#pragma unroll
      for (int ks = 0; ks < 4; ++ks) { kvacc = mfma16(afr[ks], bfr[ks], kvacc); zacc = mfma16(afr[ks], ones, zacc); }
      asm volatile("" : "+v"(kvacc), "+v"(zacc));
      SCHED();
      passB<5, 1>(g);
      const u32 pb = WS_PART + (u32)(((pj * 8 + c.w) * 16 + t) * 640);
      __builtin_amdgcn_raw_buffer_store_b64(v2u{pk2(kvacc[0], kvacc[1]), pk2(kvacc[2], kvacc[3])}, c.rs, (int)(pb + (u32)lane * 8u), 0, AUX_SC1);
      __builtin_amdgcn_raw_buffer_store_b128(__builtin_bit_cast(v4u, zacc), c.rs, (int)((lane & 15) < 2 ? pb + 512u + (u32)((lane & 1) * 64 + g4 * 16) : OOB), 0, AUX_SC1);
    }
    STAMP(2);
#define LW(i) load_w2<i>(c, wr)
    LW(18); passB<6, 3>(g); LW(19); passB<9, 3>(g); LW(20); passB<12, 2>(g);
    asm volatile("s_waitcnt vmcnt(3)" ::: "memory");
    raise_wave_flag(c, kvflag, tag);
    STAMP(3);
    BAR_L();
    issue_w2<4>(c); passB<14, 3>(g); issue_w2<5>(c); passB<17, 3>(g); issue_w2<6>(c); passB<20, 2>(g); issue_w2<7>(c); passB<22, 3>(g);
    issue_w2<8>(c); passB<25, 3>(g); issue_w2<9>(c); passB<28, 2>(g); LW(21); passB<30, 2>(g);
    BAR_L();
    STAMP(12);
    {
      const f16v pq = phi_mm(g.acc[2]);
      spq = hedgehog<true>(pq, ph, [&](int i, f2& v) __attribute__((always_inline)) {
        OPAQUE(v);
        switch (i) { case 0: issue_w2<10>(c); break; case 1: issue_w2<11>(c); break; case 2: issue_w2<12>(c); break; case 3: issue_w2<13>(c); break;
                     case 4: issue_w2<14>(c); break; case 5: issue_w2<15>(c); break; case 6: issue_w2<16>(c); break; default: issue_w2<17>(c); }
      });
      asm volatile("" : "+v"(ph[0]), "+v"(ph[1]), "+v"(ph[2]), "+v"(ph[3]));
    }
    STAMP(16);
    if (c.w < 2) poll_at(c, WS_FKV + (u32)((pj * 128 + c.w * 64 + lane) * 8), tag);
    asm volatile("s_waitcnt vmcnt(0)\n\ts_barrier" ::: "memory");
    STAMP(4);
    {
      v4u pv[8];
      const u32 pb = WS_PART + (u32)((pj * 8 + c.w) * 16 * 640);
#pragma unroll
      for (int tt = 0; tt < 8; ++tt) pv[tt] = gload_sc1(ws + tt * 640, pb + (u32)(h * 8 * 640 + tok * 16));
      v4u zv = gload_sc1(ws, pb + (u32)((lane & 15) * 640 + 512 + g4 * 16));
      fl1 = fetch_flag(c, h1off);
      const u32 one = tok < 8 ? 0x3F80u << ((tok & 1) * 16) : 0u;
      const v4u sel = {(tok >> 1) == 0 ? one : 0u, (tok >> 1) == 1 ? one : 0u, (tok >> 1) == 2 ? one : 0u, (tok >> 1) == 3 ? one : 0u};
      asm volatile("s_waitcnt vmcnt(1)" : "+v"(pv[0]), "+v"(pv[1]), "+v"(pv[2]), "+v"(pv[3]), "+v"(pv[4]), "+v"(pv[5]), "+v"(pv[6]), "+v"(pv[7]), "+v"(zv) :: "memory");
      f16v sum = zero16;
#pragma unroll
      for (int tt = 0; tt < 8; ++tt) sum = mfma32(sel, pv[tt], sum);
      {
        const int pl = 2 * tok + h;
        sts64(L_KVT + (u32)(((c.w >> 1) * 16 + (pl & 15)) * 64 + (c.w & 1) * 32 + (pl >> 4) * 8), v2u{pk2(sum[0], sum[1]), pk2(sum[2], sum[3])});
      }
      f4 z = __builtin_bit_cast(f4, zv);
#pragma unroll
      for (int e = 0; e < 4; ++e) z[e] = dpp_add<0x140>(dpp_add<0x141>(dpp_add<0x4E>(dpp_add<0xB1>(z[e]))));
      if ((lane & 15) == 0) sts64(L_ZT + (u32)(c.w * 32 + g4 * 8), v2u{pk2(z[0], z[1]), pk2(z[2], z[3])});
      STAMP(17);
    }
    check_flag<0>(c, fl1, h1off, tag);
    p2_early<0, 6>(c, mt, af);
    BAR_L();
    STAMP(20);
    {
      const int i = lane & 31, hd = (i >> 2) & 1, d = (i & 3) + 4 * (i >> 3);
      const u32 kad = on ? L_KVT + (u32)(((2 * wn + hd) * 16 + d) * 64) : L_Z0, zad = on ? L_ZT + (u32)((2 * wn + hd) * 64) : L_Z0;
      f16v num = zero16, den = zero16;
#pragma unroll
      for (int s = 0; s < 4; ++s) {
        const v4u ka = lds128(kad + (u32)(s * 16)), za = lds128(zad + (u32)(s * 16));
        num = mfma32(ka, ph[s], num);
        den = mfma32(za, ph[s], den);
      }
      const u32 gbits = g.st.w;
      const float sc = __builtin_bit_cast(float, gbits) * __builtin_amdgcn_rcpf(__builtin_fmaf(spq, 1e-6f, den[0]));
#pragma unroll
      for (int e = 0; e < 16; ++e) num[e] *= sc;
      const u32 ob = abase + (u32)(86 + 4 * pj + 2 * wn) * 1024u;
      __builtin_amdgcn_raw_buffer_store_b128(pack8(num, 0), c.rs, (int)ob, 0, AUX_SC1);
      __builtin_amdgcn_raw_buffer_store_b128(pack8(num, 1), c.rs, (int)(ob + 1024u), 0, AUX_SC1);
    }
    STAMP(5);
    phase2<0>(c, mt, nt, out, af, wr TS_ARG);
  } else {
    c.wb = (const u8*)w1 + tile0 * 64;
    c.woff = (u32)(xr * 2752) + ch;
    c.woffB = (u32)(xr * 2752) + (six ? ch : (ch & ~64u));
    if (c.w >= 2) { tb = (const u8*)(b1 + tile0 * 32); tdst = L_T3 + (u32)(c.w & 1) * 1024u; tlim = six ? 23 : 19; }
    STAMP(11);
    passA<1>(c, g, tb, tdst, tlim, xrow, (const u8*)gw);
    STAMP(1);
    const f4 stf = __builtin_bit_cast(f4, g.st);
    const u64 tag = read_tag();
    const u32 hb = abase + (u32)(2 * (tile0 + wn)) * 1024u, h1flag = WS_FH1 + (u32)((((t * 4 + wm) * 8 + pj) * 2 + wn) * 8);
    const float rstd = __builtin_amdgcn_rcpf(stf[1]), io = stf[2];
    v4u lnb;
    {
      const u32 hi = pk2(stf[0], stf[1]), lo = pk2(stf[0] - bf2f(hi & 0xFFFFu), stf[1] - bfhi(hi));
      lnb = v4u{(hi & 0xFFFFu) | (hi << 16), (lo & 0xFFFFu) | (hi & 0xFFFF0000u), lo >> 16, 0u};
    }
    ln_fold(c, g.acc[0], 0, lnb); ln_fold(c, g.acc[1], 1, lnb);
    issue_w2s<4, 2>(c);
    gelu_half(c, g.acc[0], 0, rstd, io, hb); gelu_half(c, g.acc[0], 1, rstd, io, hb + 1024u);
    issue_w2s<6, 1>(c);
    gelu_half(c, g.acc[1], 0, rstd, io, hb + 4096u);
    rdB<0>(g); rdB<1>(g); rdB<2>(g);
    gelu_half(c, g.acc[1], 1, rstd, io, hb + 5120u);
    STAMP(2);
#pragma unroll
    for (int e = 0; e < 16; ++e) g.acc[2][e] = 0.f;
    ln_fold(c, g.acc[2], 2, lnb);
    __builtin_amdgcn_s_barrier();
    SCHED();
    passB<0, 2>(g); issue_w2<7>(c); passB<2, 3>(g); issue_w2<8>(c); passB<5, 3>(g); issue_w2<9>(c); passB<8, 3>(g);
    LW(18); passB<11, 3>(g); LW(19); passB<14, 3>(g);
    asm volatile("s_waitcnt vmcnt(5)" ::: "memory");
    raise_wave_flag(c, h1flag, tag);
    STAMP(3);
    LW(20); passB<17, 3>(g); LW(21); passB<20, 3>(g); LW(22); passB<23, 2>(g);
    BAR_L();
    issue_w2<10>(c); passB<25, 2>(g); issue_w2<11>(c); passB<27, 2>(g); issue_w2<12>(c); passB<29, 3>(g); issue_w2<13>(c);
    STAMP(12);
    poll_at(c, h1off, tag);
    p2_early<0, 6>(c, mt, af);
    STAMP(4);
    const u32 hb2 = (six || wn == 0) ? hb + 8192u : OOB - 1024u;
    gelu_half(c, g.acc[2], 0, rstd, io, hb2);
    gelu_half(c, g.acc[2], 1, rstd, io, hb2 + 1024u);
    STAMP(5);
    phase2<1>(c, mt, nt, out, af, wr TS_ARG);
  }

}

}

extern "C" void kernel_launch(void* const* d_in, const int* in_sizes, int n_in, void* d_out, int out_size, void* d_ws, size_t ws_size,
                              hipStream_t stream) {
  (void)in_sizes; (void)n_in; (void)out_size; (void)ws_size;
  (void)hipFuncSetAttribute((const void*)fused_kernel, hipFuncAttributeMaxDynamicSharedMemorySize, LDS_BYTES);
  fused_kernel<<<256, 512, LDS_BYTES, stream>>>((const u16*)d_in[0], (const u16*)d_in[1], (const u16*)d_in[2], (const u16*)d_in[3],
                                                (const u16*)d_in[5], (const u16*)d_in[6], (const u16*)d_in[7], (const u16*)d_in[8],
                                                (const u16*)d_in[9], (u16*)d_out, (u8*)d_ws);
}
```
